```python
import torch

def _softmax(x, axis=-1):
    return torch.softmax(x, dim=axis)
def _gelu(x):
    return torch.nn.functional.gelu(x, approximate="tanh")
def _silu(x):
    return torch.nn.functional.silu(x)
def _sigmoid(x):
    return torch.sigmoid(x)
def _layernorm(x, axis=-1, eps=1e-5):
    m = x.mean(dim=axis, keepdim=True)
    v = x.var(dim=axis, keepdim=True, unbiased=False)
    return (x - m) / torch.sqrt(v + eps)
def _scatter_add(tgt, idx, src, axis=0):
    return tgt.clone().index_add_(axis, idx, src)
def _segment_sum(src, seg, n):
    out = torch.zeros((n,) + src.shape[1:], dtype=src.dtype)
    return out.index_add_(0, seg, src)
def _pad(x, pw):
    flat = tuple(v for pair in reversed(pw) for v in pair)
    return torch.nn.functional.pad(x, flat)

kernel_name = 'concat_heads__mha_block_cached__sliding_block_cached__decode'

def setup_inputs():

    _w = (lambda a: (lambda t: t.to(torch.bfloat16) if t.is_floating_point() else t)(torch.as_tensor(a)))
    def _A_setup():
        B, Sq, D = 2, 1, 640
        H, Sk, Dh = 32, 4096, 192
        return {
            "x": _w((lambda *s: torch.randn(*s, dtype=torch.bfloat16) * 0.4)(B, Sq, D)),
            "k_cache": _w((lambda *s: torch.randn(*s, dtype=torch.bfloat16) * 3.2)(B, H, Sk, Dh)),
            "v_cache": _w((lambda *s: torch.randn(*s, dtype=torch.bfloat16) * 0.8)(B, H, Sk, Dh)),
            "wq": _w((lambda *s: torch.randn(*s, dtype=torch.bfloat16) * 3.2)(D, H * Dh) * (D ** -0.5)),
            "wo": _w((lambda *s: torch.randn(*s, dtype=torch.bfloat16) * 0.8)(H * Dh, D) * ((H * Dh) ** -0.5)),
        }
    _A_in = _A_setup()

    def _B_setup():
        B, Sq, D = 2, 1, 640
        Hq, Hkv, Sk, Dh = 32, 32, 4096, 192
        assert Hq % Hkv == 0, (Hq, Hkv)
        return {
            "x": _w((lambda *s: torch.randn(*s, dtype=torch.bfloat16) * 0.2)(B, Sq, D)),
            "k_cache": _w((lambda *s: torch.randn(*s, dtype=torch.bfloat16) * 3.2)(B, Hkv, Sk, Dh)),
            "v_cache": _w((lambda *s: torch.randn(*s, dtype=torch.bfloat16) * 1.6)(B, Hkv, Sk, Dh)),
            "wq": _w((lambda *s: torch.randn(*s, dtype=torch.bfloat16) * 3.2)(D, Hq * Dh) * (D ** -0.5)),
            "wo": _w((lambda *s: torch.randn(*s, dtype=torch.bfloat16) * 1.6)(Hq * Dh, D) * ((Hq * Dh) ** -0.5)),
        }
    _B_in = _B_setup()

    return {
        "A_x": _A_in["x"],
        "A_k_cache": _A_in["k_cache"],
        "A_v_cache": _A_in["v_cache"],
        "A_wq": _A_in["wq"],
        "A_wo": _A_in["wo"],
        "B_x": _B_in["x"],
        "B_k_cache": _B_in["k_cache"],
        "B_v_cache": _B_in["v_cache"],
        "B_wq": _B_in["wq"],
        "B_wo": _B_in["wo"],
    }

def reference(A_x, A_k_cache, A_v_cache, A_wq, A_wo, B_x, B_k_cache, B_v_cache, B_wq, B_wo):
    def _A_ref(x, k_cache, v_cache, wq, wo):
        B, Sq, D = x.shape
        H, Dh = 32, 192
        q = (lambda x, *d: x.permute(*d))((x @ wq).reshape(B, Sq, H, Dh), 0, 2, 1, 3)
        scores = torch.einsum("bhqd,bhkd->bhqk", q, k_cache) * (Dh ** -0.5)
        p = _softmax(scores, axis=-1)
        o = torch.einsum("bhqk,bhkd->bhqd", p, v_cache)
        return (lambda x, *d: x.permute(*d))(o, 0, 2, 1, 3).reshape(B, Sq, H * Dh) @ wo

    def _B_ref(x, k_cache, v_cache, wq, wo):
        B, Sq, D = x.shape
        Hq, Dh = 32, 192
        Hkv, Sk = k_cache.shape[1], k_cache.shape[2]
        W = min(4096, max(256, Sk // 4))
        rep = Hq // Hkv
        q = (lambda x, *d: x.permute(*d))((x @ wq).reshape(B, Sq, Hq, Dh), 0, 2, 1, 3)
        k = (lambda x, n, axis: torch.repeat_interleave(x, n, dim=axis))(k_cache[:, :, Sk - W:, :], rep, axis=1)
        v = (lambda x, n, axis: torch.repeat_interleave(x, n, dim=axis))(v_cache[:, :, Sk - W:, :], rep, axis=1)
        scores = torch.einsum("bhqd,bhkd->bhqk", q, k) * (Dh ** -0.5)
        p = _softmax(scores, axis=-1)
        o = torch.einsum("bhqk,bhkd->bhqd", p, v)
        return (lambda x, *d: x.permute(*d))(o, 0, 2, 1, 3).reshape(B, Sq, Hq * Dh) @ wo

    a = _A_ref(A_x, A_k_cache, A_v_cache, A_wq, A_wo)
    b = _B_ref(B_x, B_k_cache, B_v_cache, B_wq, B_wo)
    return torch.concatenate([a, b], axis=-1)
```

```cpp
#include <hip/hip_runtime.h>
#include <hip/hip_bf16.h>
#include <stdint.h>

#define MIMIC 0

typedef unsigned u32x4 __attribute__((ext_vector_type(4)));
typedef unsigned u32x2 __attribute__((ext_vector_type(2)));
typedef float f32x4 __attribute__((ext_vector_type(4)));
typedef short bf16x8 __attribute__((ext_vector_type(8)));
typedef __bf16 bf16x2 __attribute__((ext_vector_type(2)));

constexpr int NH = 32, DH = 192, DM = 640, SK = 4096, WIN = 1024;
constexpr int ROWB = DH * 2;
constexpr int NWAVE = 10, NTHR = NWAVE * 64;

constexpr size_t WS_QP = 0;
constexpr size_t WS_QP_BYTES = (size_t)NH * 2 * 4 * 2 * DH * 4;
constexpr size_t WS_PO = WS_QP + WS_QP_BYTES;
constexpr size_t WS_PO_BYTES = (size_t)NH * 2 * 2 * DH * 4 * 4;
constexpr size_t WS_ML = WS_PO + WS_PO_BYTES;
constexpr size_t WS_ML_BYTES = (size_t)NH * 2 * 2 * 4 * 2 * 4;
constexpr size_t WS_Y = WS_ML + WS_ML_BYTES;
constexpr size_t WS_Y_BYTES = (size_t)2 * 2 * 64 * 2 * 320 * 4;

struct Args {
  const unsigned char* x[2];
  const unsigned char* k[2];
  const unsigned char* v[2];
  const unsigned char* wq[2];
  const unsigned char* wo[2];
  unsigned char* out;
  unsigned char* ws;
};

__device__ __forceinline__ float dot2(unsigned a, unsigned b, float c) {
  return __builtin_amdgcn_fdot2_f32_bf16(__builtin_bit_cast(bf16x2, a), __builtin_bit_cast(bf16x2, b), c, false);
}
__device__ __forceinline__ unsigned cvtpk(float lo, float hi) {
  typedef float f32x2_ __attribute__((ext_vector_type(2)));
  const f32x2_ v = {lo, hi};
  return __builtin_bit_cast(unsigned, __builtin_convertvector(v, bf16x2));
}
template <int CTRL> __device__ __forceinline__ float dpp(float x) {
  return __builtin_bit_cast(float, __builtin_amdgcn_mov_dpp(__builtin_bit_cast(int, x), CTRL, 0xf, 0xf, true));
}
constexpr int XOR1 = 0xB1, XOR2 = 0x4E, HMIR = 0x141, ROR8 = 0x128, ROR4 = 0x124;
__device__ __forceinline__ float xrow_max(float x) {
  auto s = __builtin_amdgcn_permlane16_swap(__float_as_uint(x), __float_as_uint(x), false, false);
  x = fmaxf(__uint_as_float(s[0]), __uint_as_float(s[1]));
  auto t = __builtin_amdgcn_permlane32_swap(__float_as_uint(x), __float_as_uint(x), false, false);
  return fmaxf(__uint_as_float(t[0]), __uint_as_float(t[1]));
}
__device__ __forceinline__ float xrow_sum(float x) {
  auto s = __builtin_amdgcn_permlane16_swap(__float_as_uint(x), __float_as_uint(x), false, false);
  x = __uint_as_float(s[0]) + __uint_as_float(s[1]);
  auto t = __builtin_amdgcn_permlane32_swap(__float_as_uint(x), __float_as_uint(x), false, false);
  return __uint_as_float(t[0]) + __uint_as_float(t[1]);
}
__device__ __forceinline__ u32x4 ldnt(const unsigned char* p) {
  return __builtin_nontemporal_load(reinterpret_cast<const u32x4*>(p));
}
__device__ __forceinline__ f32x4 mfma(u32x4 a, u32x4 b, f32x4 c) {
  return __builtin_amdgcn_mfma_f32_16x16x32_bf16(__builtin_bit_cast(bf16x8, a), __builtin_bit_cast(bf16x8, b), c, 0, 0, 0);
}
__device__ __forceinline__ u32x4 lane_mask(int lane) {
  const int j = lane & 7;
  const unsigned hm = (j & 1) ? 0xFFFF0000u : 0x0000FFFFu;
  u32x4 m;
  m[0] = (j >> 1) == 0 ? hm : 0u; m[1] = (j >> 1) == 1 ? hm : 0u; m[2] = (j >> 1) == 2 ? hm : 0u; m[3] = (j >> 1) == 3 ? hm : 0u;
  return m;
}
__device__ __forceinline__ u32x4 afrag(unsigned pp, u32x4 mask) {
  u32x4 a; a[0] = pp & mask[0]; a[1] = pp & mask[1]; a[2] = pp & mask[2]; a[3] = pp & mask[3]; return a;
}
__device__ __forceinline__ float bf16r(float x) {
  return __uint_as_float(cvtpk(x, x) << 16);
}

__global__ void __launch_bounds__(NTHR) k_qproj(Args a) {
  __shared__ __attribute__((aligned(16))) float lds[NWAVE * 2 * 2 * DH];
  const int h = blockIdx.x & 31, j = blockIdx.x >> 5, p = j >> 2, qt = j & 3;
  const int tid = threadIdx.x, lane = tid & 63, w = __builtin_amdgcn_readfirstlane(tid >> 6);
  const u32x4 mask = lane_mask(lane);
  const int row0 = qt * 160 + w * 16;
  const unsigned char* wq = a.wq[p] + (size_t)row0 * (NH * ROWB) + h * ROWB;
  const unsigned loff = (lane >> 3) * (NH * ROWB) + (lane & 7) * 16;
  u32x4 W[2][3];
#pragma unroll
  for (int u = 0; u < 2; ++u)
#pragma unroll
    for (int t = 0; t < 3; ++t) W[u][t] = ldnt(wq + (size_t)u * 8 * (NH * ROWB) + loff + t * 128);
  unsigned xv[2][2];
#pragma unroll
  for (int b = 0; b < 2; ++b)
#pragma unroll
    for (int u = 0; u < 2; ++u) {
      const unsigned short xs = *reinterpret_cast<const unsigned short*>(a.x[p] + ((size_t)b * DM + row0 + u * 8 + (lane >> 3)) * 2);
      xv[b][u] = (unsigned)xs * 0x10001u;
    }
  f32x4 acc[2][3];
#pragma unroll
  for (int b = 0; b < 2; ++b)
#pragma unroll
    for (int t = 0; t < 3; ++t) acc[b][t] = f32x4{0.f, 0.f, 0.f, 0.f};
#pragma unroll
  for (int u = 0; u < 2; ++u)
#pragma unroll
    for (int b = 0; b < 2; ++b) {
      const u32x4 A = afrag(xv[b][u], mask);
#pragma unroll
      for (int t = 0; t < 3; ++t) acc[b][t] = mfma(A, W[u][t], acc[b][t]);
    }
  const bool valid = ((lane >> 5) == ((lane >> 3) & 1));
  if (valid) {
    const int half = lane >> 5, d0 = 8 * (lane & 7) + 4 * ((lane >> 4) & 1);
#pragma unroll
    for (int b = 0; b < 2; ++b)
#pragma unroll
      for (int t = 0; t < 3; ++t) *reinterpret_cast<f32x4*>(&lds[((w * 2 + b) * 2 + half) * DH + 64 * t + d0]) = acc[b][t];
  }
  __syncthreads();
  if (tid < 2 * DH) {
    const int b = tid / DH, d = tid - b * DH;
    float s = 0.f;
#pragma unroll
    for (int ww = 0; ww < NWAVE; ++ww) s += lds[((ww * 2 + b) * 2 + 0) * DH + d] + lds[((ww * 2 + b) * 2 + 1) * DH + d];
    float* qp = reinterpret_cast<float*>(a.ws + WS_QP);
    qp[(((h * 2 + p) * 4 + qt) * 2 + b) * DH + d] = s;
  }
}

__global__ void __launch_bounds__(NTHR) k_attn(Args a) {
  __shared__ __attribute__((aligned(16))) float lds[NWAVE * 2 * DH + 64];
  __shared__ __attribute__((aligned(16))) unsigned short qs[2 * DH];
  const int h = blockIdx.x & 31, j = blockIdx.x >> 5, b = j >> 2, s = j & 3;
  const int tid = threadIdx.x, lane = tid & 63, w = __builtin_amdgcn_readfirstlane(tid >> 6);
  const int p = w >= 8 ? 1 : 0;
  const int key0 = p ? (SK - WIN) + s * 256 + (w - 8) * 128 : s * 1024 + w * 128;
  const size_t base = ((size_t)(b * NH + h) * SK + key0) * ROWB;
  const unsigned char* kp = a.k[p] + base;
  const unsigned char* vp = a.v[p] + base;
  const unsigned loff = (lane >> 3) * ROWB + (lane & 7) * 16;
  u32x4 K[2][2][3], V[2][2][3];
#define LOADK(slot, tile) _Pragma("unroll") for (int u = 0; u < 2; ++u) _Pragma("unroll") for (int t = 0; t < 3; ++t) \
    K[slot][u][t] = ldnt(kp + (size_t)(tile) * 16 * ROWB + loff + u * 8 * ROWB + t * 128);
#define LOADV(slot, tile) _Pragma("unroll") for (int u = 0; u < 2; ++u) _Pragma("unroll") for (int t = 0; t < 3; ++t) \
    V[slot][u][t] = ldnt(vp + (size_t)(tile) * 16 * ROWB + loff + u * 8 * ROWB + t * 128);
  LOADK(0, 0) LOADV(0, 0) LOADK(1, 1) LOADV(1, 1)
  if (tid < 2 * DH) {
    const int pp = tid / DH, d = tid - pp * DH;
    const float* qp = reinterpret_cast<const float*>(a.ws + WS_QP);
    float sum = 0.f;
#pragma unroll
    for (int qt = 0; qt < 4; ++qt) sum += qp[(((h * 2 + pp) * 4 + qt) * 2 + b) * DH + d];
    qs[tid] = (unsigned short)(cvtpk(sum, sum) & 0xFFFFu);
  }
  __syncthreads();
  u32x4 q[3];
#pragma unroll
  for (int t = 0; t < 3; ++t) q[t] = *reinterpret_cast<const u32x4*>(&qs[p * DH + 64 * t + 8 * (lane & 7)]);
  const u32x4 mask = lane_mask(lane);
  constexpr float SCALE = 0.07216878364870322f, LOG2E = 1.4426950408889634f;
  float m = -INFINITY, l = 0.f;
  f32x4 acc[3];
#pragma unroll
  for (int t = 0; t < 3; ++t) acc[t] = f32x4{0.f, 0.f, 0.f, 0.f};
#pragma unroll
  for (int it = 0; it < 8; ++it) {
    const int sl = it & 1;
    float sc[2];
#pragma unroll
    for (int u = 0; u < 2; ++u) {
      float d = 0.f;
#pragma unroll
      for (int t = 0; t < 3; ++t)
#pragma unroll
        for (int e = 0; e < 4; ++e) d = dot2(K[sl][u][t][e], q[t][e], d);
      d += dpp<XOR1>(d); d += dpp<XOR2>(d); d += dpp<HMIR>(d);
      sc[u] = d * (SCALE * LOG2E);
    }
    if (it + 2 < 8) { LOADK(sl, it + 2) }
    float tm = fmaxf(sc[0], sc[1]);
    tm = fmaxf(tm, dpp<ROR8>(tm)); tm = xrow_max(tm);
    const float mn = fmaxf(m, tm);
    const float alpha = __builtin_amdgcn_exp2f(m - mn);
    m = mn;
    const float p0 = __builtin_amdgcn_exp2f(sc[0] - mn), p1 = __builtin_amdgcn_exp2f(sc[1] - mn);
    l = fmaf(l, alpha, p0 + p1);
#pragma unroll
    for (int t = 0; t < 3; ++t) acc[t] *= alpha;
    const u32x4 A0 = afrag(cvtpk(p0, p0), mask), A1 = afrag(cvtpk(p1, p1), mask);
#pragma unroll
    for (int t = 0; t < 3; ++t) acc[t] = mfma(A0, V[sl][0][t], acc[t]);
#pragma unroll
    for (int t = 0; t < 3; ++t) acc[t] = mfma(A1, V[sl][1][t], acc[t]);
    if (it + 2 < 8) { LOADV(sl, it + 2) }
  }
  l += dpp<ROR8>(l); l = xrow_sum(l);
  const bool valid = ((lane >> 5) == ((lane >> 3) & 1));
  if (valid) {
    const int half = lane >> 5, d0 = 8 * (lane & 7) + 4 * ((lane >> 4) & 1);
#pragma unroll
    for (int t = 0; t < 3; ++t) *reinterpret_cast<f32x4*>(&lds[(w * 2 + half) * DH + 64 * t + d0]) = acc[t];
  }
  float* ml = lds + NWAVE * 2 * DH;
  if (lane == 0) { ml[2 * w] = m; ml[2 * w + 1] = l; }
  __syncthreads();
  if (tid < 2 * DH) {
    const int pp = tid / DH, d = tid - pp * DH;
    const int w0 = pp ? 8 : 0, nw = pp ? 2 : 8;
    float M = -INFINITY;
    for (int ww = 0; ww < nw; ++ww) M = fmaxf(M, ml[2 * (w0 + ww)]);
    float L = 0.f, o = 0.f;
    for (int ww = 0; ww < nw; ++ww) {
      const float wt = __builtin_amdgcn_exp2f(ml[2 * (w0 + ww)] - M);
      L = fmaf(wt, ml[2 * (w0 + ww) + 1], L);
      o = fmaf(wt, lds[((w0 + ww) * 2 + 0) * DH + d] + lds[((w0 + ww) * 2 + 1) * DH + d], o);
    }
    float* po = reinterpret_cast<float*>(a.ws + WS_PO);
    po[(((h * 2 + pp) * 2 + b) * DH + d) * 4 + s] = o;
    if (d == 0) {
      float* pml = reinterpret_cast<float*>(a.ws + WS_ML);
      pml[(((h * 2 + pp) * 2 + b) * 4 + s) * 2] = M;
      pml[(((h * 2 + pp) * 2 + b) * 4 + s) * 2 + 1] = L;
    }
  }
}

__global__ void __launch_bounds__(NTHR) k_oproj(Args a) {
  __shared__ __attribute__((aligned(16))) float lds[NWAVE * 2 * 2 * 64];
  __shared__ __attribute__((aligned(16))) unsigned short os[2 * 96];
  const int h = blockIdx.x & 31, j = blockIdx.x >> 5, p = j >> 2, rh = (j >> 1) & 1, ch = j & 1;
  const int tid = threadIdx.x, lane = tid & 63, w = __builtin_amdgcn_readfirstlane(tid >> 6);
  const int lc = w % 5, rq = w / 5;
  const unsigned char* wo = a.wo[p] + (size_t)(h * DH + rh * 96 + rq * 48) * (DM * 2) + ch * 640 + lc * 128;
  const unsigned loff = (lane >> 3) * (DM * 2) + (lane & 7) * 16;
  u32x4 W[6];
#pragma unroll
  for (int u = 0; u < 6; ++u) W[u] = ldnt(wo + (size_t)u * 8 * (DM * 2) + loff);
  if (tid < 192) {
    const int b = tid / 96, r = tid - b * 96;
    const float* po = reinterpret_cast<const float*>(a.ws + WS_PO);
    const float* pml = reinterpret_cast<const float*>(a.ws + WS_ML);
    const f32x4 o4 = *reinterpret_cast<const f32x4*>(po + (((h * 2 + p) * 2 + b) * DH + rh * 96 + r) * 4);
    const f32x4 ml0 = *reinterpret_cast<const f32x4*>(pml + ((h * 2 + p) * 2 + b) * 8);
    const f32x4 ml1 = *reinterpret_cast<const f32x4*>(pml + ((h * 2 + p) * 2 + b) * 8 + 4);
    const float M = fmaxf(fmaxf(ml0[0], ml0[2]), fmaxf(ml1[0], ml1[2]));
    const float w0 = __builtin_amdgcn_exp2f(ml0[0] - M), w1 = __builtin_amdgcn_exp2f(ml0[2] - M);
    const float w2 = __builtin_amdgcn_exp2f(ml1[0] - M), w3 = __builtin_amdgcn_exp2f(ml1[2] - M);
    const float L = w0 * ml0[1] + w1 * ml0[3] + w2 * ml1[1] + w3 * ml1[3];
    const float o = (w0 * o4[0] + w1 * o4[1] + w2 * o4[2] + w3 * o4[3]) / L;
    os[tid] = (unsigned short)(cvtpk(o, o) & 0xFFFFu);
  }
  __syncthreads();
  const u32x4 mask = lane_mask(lane);
  f32x4 acc[2] = {f32x4{0.f, 0.f, 0.f, 0.f}, f32x4{0.f, 0.f, 0.f, 0.f}};
#pragma unroll
  for (int u = 0; u < 6; ++u)
#pragma unroll
    for (int b = 0; b < 2; ++b) {
      const unsigned ov = os[b * 96 + rq * 48 + u * 8 + (lane >> 3)];
      acc[b] = mfma(afrag(ov * 0x10001u, mask), W[u], acc[b]);
    }
  const bool valid = ((lane >> 5) == ((lane >> 3) & 1));
  if (valid) {
    const int half = lane >> 5, d0 = 8 * (lane & 7) + 4 * ((lane >> 4) & 1);
#pragma unroll
    for (int b = 0; b < 2; ++b) *reinterpret_cast<f32x4*>(&lds[((w * 2 + b) * 2 + half) * 64 + d0]) = acc[b];
  }
  __syncthreads();
  {
    const int b = tid / 320, n = tid - b * 320, c = n >> 6, e = n & 63;
    const float y = (lds[((c * 2 + b) * 2 + 0) * 64 + e] + lds[((c * 2 + b) * 2 + 1) * 64 + e])
                  + (lds[(((c + 5) * 2 + b) * 2 + 0) * 64 + e] + lds[(((c + 5) * 2 + b) * 2 + 1) * 64 + e]);
    float* Y = reinterpret_cast<float*>(a.ws + WS_Y);
    Y[(((p * 2 + ch) * 64 + h * 2 + rh) * 2 + b) * 320 + n] = y;
  }
}

__global__ void __launch_bounds__(64) k_final(Args a) {
  const int un = blockIdx.x;
  const int cg = un % 20, b = (un / 20) & 1, pc = un / 40;
  const int lane = threadIdx.x;
  const float* Y = reinterpret_cast<const float*>(a.ws + WS_Y);
  f32x4 s = f32x4{0.f, 0.f, 0.f, 0.f};
#pragma unroll
  for (int i = 0; i < 4; ++i)
    s += *reinterpret_cast<const f32x4*>(Y + ((size_t)(pc * 64 + (lane >> 2) + 16 * i) * 2 + b) * 320 + cg * 16 + (lane & 3) * 4);
#pragma unroll
  for (int e = 0; e < 4; ++e) {
    float x = s[e];
    x += dpp<ROR8>(x); x += dpp<ROR4>(x); x = xrow_sum(x);
    s[e] = x;
  }
  if (lane < 4) {
    u32x2 o; o[0] = cvtpk(s[0], s[1]); o[1] = cvtpk(s[2], s[3]);
    *reinterpret_cast<u32x2*>(a.out + ((size_t)b * 1280 + pc * 320 + cg * 16 + lane * 4) * 2) = o;
  }
}

extern "C" void kernel_launch(void* const* d_in, const int* in_sizes, int n_in,
                              void* d_out, int out_size, void* d_ws, size_t ws_size,
                              hipStream_t stream) {
  Args a;
  for (int p = 0; p < 2; ++p) {
    a.x[p] = (const unsigned char*)d_in[5 * p + 0];
    a.k[p] = (const unsigned char*)d_in[5 * p + 1];
    a.v[p] = (const unsigned char*)d_in[5 * p + 2];
    a.wq[p] = (const unsigned char*)d_in[5 * p + 3];
    a.wo[p] = (const unsigned char*)d_in[5 * p + 4];
  }
  a.out = (unsigned char*)d_out;
  a.ws = (unsigned char*)d_ws;
  k_qproj<<<256, NTHR, 0, stream>>>(a);
  k_attn<<<256, NTHR, 0, stream>>>(a);
  k_oproj<<<256, NTHR, 0, stream>>>(a);
  k_final<<<160, 64, 0, stream>>>(a);
}
```

```cpp
#include <hip/hip_runtime.h>
#include <hip/hip_bf16.h>
#include <stdint.h>

#define WQBURST 5
#define TLBWARM 0
#define STG 1
#define BSWAP 1
#define STAMPS 0

typedef unsigned u32x4 __attribute__((ext_vector_type(4)));
typedef unsigned u32x2 __attribute__((ext_vector_type(2)));
typedef float f32x4 __attribute__((ext_vector_type(4)));
typedef float f32x2 __attribute__((ext_vector_type(2)));
typedef short bf16x8 __attribute__((ext_vector_type(8)));
typedef __bf16 bf16x2 __attribute__((ext_vector_type(2)));
typedef __amdgpu_buffer_rsrc_t rsrc_t;
#define LAS __attribute__((address_space(3)))

constexpr int NH = 32, DH = 192, DM = 640, SK = 4096;
constexpr unsigned ROWB = DH * 2;
constexpr unsigned SEGB = SK * ROWB;
constexpr unsigned KVB = 2u * NH * SEGB;
constexpr unsigned WQB = DM * NH * ROWB;
constexpr unsigned TILEB = 8 * ROWB;
constexpr unsigned RROW = 32 * TILEB;
constexpr int NTHR = 320;
constexpr int AUX_NT = 2, AUX_SC1 = 16;
constexpr unsigned EMPTY = 0xFFFFFFFFu;
constexpr unsigned SPIN_CAP = 1u << 16;

constexpr unsigned WS_EPOCH = 0;
constexpr unsigned WS_FLAG = 256;
constexpr unsigned WS_QP = 4096;
constexpr unsigned WS_PO = WS_QP + 256 * 2 * DH * 4;
constexpr unsigned WS_ML = WS_PO + NH * 4 * 8 * DH * 4;
constexpr unsigned WS_Y = WS_ML + NH * 4 * 8 * 8;
constexpr unsigned WS_END = WS_Y + 4 * 64 * 640 * 4;
constexpr unsigned WS_DBG = 4u << 20;

constexpr int L_PART = 0;
constexpr int L_ML = L_PART + 4 * 4 * 2 * DH * 4;
constexpr int L_Q = L_ML + 4 * 4 * 8;
constexpr int L_O = L_Q + 4 * DH * 2;
constexpr int L_CNT = L_O + 2 * 8 * 12 * 4;
constexpr int L_QPART = L_CNT + 64;
constexpr int L_END = L_QPART + 4 * 2 * 2 * DH * 4;
constexpr int LDS_BYTES = 96 * 1024;
static_assert(L_END <= LDS_BYTES, "");

struct Args {
  const unsigned char* x[2];
  const unsigned char* k[2];
  const unsigned char* v[2];
  const unsigned char* wq[2];
  const unsigned char* wo[2];
  unsigned char* out;
  unsigned char* ws;
};

__device__ __forceinline__ float dot2(unsigned a, unsigned b, float c) {
  return __builtin_amdgcn_fdot2_f32_bf16(__builtin_bit_cast(bf16x2, a), __builtin_bit_cast(bf16x2, b), c, false);
}
__device__ __forceinline__ unsigned cvtpk(float lo, float hi) {
  const f32x2 v = {lo, hi};
  return __builtin_bit_cast(unsigned, __builtin_convertvector(v, bf16x2));
}
template <int CTRL> __device__ __forceinline__ float dpp(float x) {
  return __builtin_bit_cast(float, __builtin_amdgcn_mov_dpp(__builtin_bit_cast(int, x), CTRL, 0xf, 0xf, true));
}
constexpr int XOR1 = 0xB1, XOR2 = 0x4E, HMIR = 0x141, ROR8 = 0x128, ROR4 = 0x124;
__device__ __forceinline__ float xrow_max(float x) {
  auto s = __builtin_amdgcn_permlane16_swap(__float_as_uint(x), __float_as_uint(x), false, false);
  x = fmaxf(__uint_as_float(s[0]), __uint_as_float(s[1]));
  auto t = __builtin_amdgcn_permlane32_swap(__float_as_uint(x), __float_as_uint(x), false, false);
  return fmaxf(__uint_as_float(t[0]), __uint_as_float(t[1]));
}
__device__ __forceinline__ float xrow_sum(float x) {
  auto s = __builtin_amdgcn_permlane16_swap(__float_as_uint(x), __float_as_uint(x), false, false);
  x = __uint_as_float(s[0]) + __uint_as_float(s[1]);
  auto t = __builtin_amdgcn_permlane32_swap(__float_as_uint(x), __float_as_uint(x), false, false);
  return __uint_as_float(t[0]) + __uint_as_float(t[1]);
}
__device__ __forceinline__ f32x4 mfma(u32x4 a, u32x4 b, f32x4 c) {
  return __builtin_amdgcn_mfma_f32_16x16x32_bf16(__builtin_bit_cast(bf16x8, a), __builtin_bit_cast(bf16x8, b), c, 0, 0, 0);
}
__device__ __forceinline__ u32x4 lane_mask(int lane) {
  const int j = lane & 7;
  const unsigned hm = (j & 1) ? 0xFFFF0000u : 0x0000FFFFu;
  u32x4 m;
  m[0] = (j >> 1) == 0 ? hm : 0u; m[1] = (j >> 1) == 1 ? hm : 0u; m[2] = (j >> 1) == 2 ? hm : 0u; m[3] = (j >> 1) == 3 ? hm : 0u;
  return m;
}
__device__ __forceinline__ u32x4 afrag(unsigned pp, u32x4 mask) {
  u32x4 a; a[0] = pp & mask[0]; a[1] = pp & mask[1]; a[2] = pp & mask[2]; a[3] = pp & mask[3]; return a;
}
__device__ __forceinline__ float bf16r(float x) { return __uint_as_float(cvtpk(x, x) << 16); }
__device__ __forceinline__ rsrc_t mkrsrc(const void* p, unsigned bytes) {
  return __builtin_amdgcn_make_buffer_rsrc(const_cast<void*>(p), 0, bytes, 0x00020000);
}
__device__ __forceinline__ u32x4 ldnt(rsrc_t r, unsigned voff, unsigned soff) { return __builtin_amdgcn_raw_buffer_load_b128(r, voff, soff, AUX_NT); }
__device__ __forceinline__ u32x4 ldsc(rsrc_t r, unsigned voff, unsigned soff) { return __builtin_amdgcn_raw_buffer_load_b128(r, voff, soff, AUX_SC1); }
__device__ __forceinline__ void stsc(u32x4 v, rsrc_t r, unsigned voff, unsigned soff) { __builtin_amdgcn_raw_buffer_store_b128(v, r, voff, soff, AUX_SC1); }
__device__ __forceinline__ unsigned canon(float x) { const unsigned u = __float_as_uint(x); return u == EMPTY ? 0x7FC00000u : u; }
__device__ __forceinline__ u32x4 canon4(f32x4 v) { u32x4 r; r[0] = canon(v[0]); r[1] = canon(v[1]); r[2] = canon(v[2]); r[3] = canon(v[3]); return r; }
__device__ __forceinline__ unsigned umax(unsigned a, unsigned b) { return a > b ? a : b; }
__device__ __forceinline__ unsigned max4(u32x4 v, unsigned m) { return umax(umax(umax(v[0], v[1]), m), umax(v[2], v[3])); }
constexpr unsigned OOB = 0x80000000u;
#define SB() __builtin_amdgcn_sched_barrier(0)
#define WAITVM(n) __builtin_amdgcn_s_waitcnt(0x0F70 | ((n) & 15) | (((n) >> 4) << 14))
#define CLOBBER() asm volatile("" ::: "memory")

__device__ __forceinline__ void lds_arrive(LAS unsigned* p, int lane) {
  __builtin_amdgcn_fence(__ATOMIC_RELEASE, "workgroup", "local");
  if (lane == 0) __hip_atomic_fetch_add(p, 1u, __ATOMIC_RELAXED, __HIP_MEMORY_SCOPE_WORKGROUP);
}
template <int SLEEP> __device__ __forceinline__ void lds_wait(LAS unsigned* p, unsigned want) {
  while (__builtin_amdgcn_readfirstlane(__hip_atomic_load(p, __ATOMIC_RELAXED, __HIP_MEMORY_SCOPE_WORKGROUP)) != want) __builtin_amdgcn_s_sleep(SLEEP);
  __builtin_amdgcn_fence(__ATOMIC_ACQUIRE, "workgroup", "local");
}

struct Soft { float m, l; f32x4 acc[3]; };

__device__ __forceinline__ void soft_reset(Soft& s) {
  s.m = -INFINITY; s.l = 0.f;
#pragma unroll
  for (int t = 0; t < 3; ++t) s.acc[t] = f32x4{0.f, 0.f, 0.f, 0.f};
}
__device__ __forceinline__ u32x4 step_qk(Soft& s, const u32x4 (&K)[3], const u32x4 (&q)[3], u32x4 mask) {
  constexpr float SCALE = 0.07216878364870322f, LOG2E = 1.4426950408889634f;
  float d0 = 0.f, d1 = 0.f;
#pragma unroll
  for (int t = 0; t < 3; ++t) { d0 = dot2(K[t][0], q[t][0], d0); d1 = dot2(K[t][1], q[t][1], d1); d0 = dot2(K[t][2], q[t][2], d0); d1 = dot2(K[t][3], q[t][3], d1); }
  float d = d0 + d1;
  d += dpp<XOR1>(d); d += dpp<XOR2>(d); d += dpp<HMIR>(d);
  const float sc = bf16r(bf16r(d) * SCALE) * LOG2E;
  if (__builtin_amdgcn_ballot_w64(!(sc <= s.m)) != 0) {
    float tm = fmaxf(sc, dpp<ROR8>(sc)); tm = xrow_max(tm);
    const float mn = fmaxf(s.m, tm);
    const float alpha = __builtin_amdgcn_exp2f(s.m - mn);
    s.m = mn; s.l *= alpha;
#pragma unroll
    for (int t = 0; t < 3; ++t) s.acc[t] *= alpha;
  }
  const float p = __builtin_amdgcn_exp2f(sc - s.m);
  s.l += p;
  return afrag(cvtpk(p, p), mask);
}
__device__ __forceinline__ void block4(Soft& s, const u32x4 (*T)[3], const u32x4 (&q)[3], u32x4 mask) {
  constexpr float SCALE = 0.07216878364870322f, LOG2E = 1.4426950408889634f;
  float sc[4];
#pragma unroll
  for (int u = 0; u < 4; ++u) {
    float d0 = 0.f, d1 = 0.f;
#pragma unroll
    for (int t = 0; t < 3; ++t) { d0 = dot2(T[2 * u][t][0], q[t][0], d0); d1 = dot2(T[2 * u][t][1], q[t][1], d1); d0 = dot2(T[2 * u][t][2], q[t][2], d0); d1 = dot2(T[2 * u][t][3], q[t][3], d1); }
    float d = d0 + d1;
    d += dpp<XOR1>(d); d += dpp<XOR2>(d); d += dpp<HMIR>(d);
    sc[u] = bf16r(bf16r(d) * SCALE) * LOG2E;
  }
  float tm = fmaxf(fmaxf(sc[0], sc[1]), fmaxf(sc[2], sc[3]));
  tm = fmaxf(tm, dpp<ROR8>(tm)); tm = xrow_max(tm);
  s.m = tm; s.l = 0.f;
#pragma unroll
  for (int t = 0; t < 3; ++t) s.acc[t] = f32x4{0.f, 0.f, 0.f, 0.f};
#pragma unroll
  for (int u = 0; u < 4; ++u) {
    const float p = __builtin_amdgcn_exp2f(sc[u] - tm);
    s.l += p;
    const u32x4 A = afrag(cvtpk(p, p), mask);
#pragma unroll
    for (int t = 0; t < 3; ++t) s.acc[t] = mfma(A, T[2 * u + 1][t], s.acc[t]);
  }
}
__device__ __forceinline__ void step_pv(Soft& s, u32x4 A, const u32x4 (&V)[3]) {
#pragma unroll
  for (int t = 0; t < 3; ++t) s.acc[t] = mfma(A, V[t], s.acc[t]);
}
__device__ __forceinline__ void soft_flush(const Soft& s, LAS unsigned char* lds, int seg, int w, int lane) {
  float l = s.l; l += dpp<ROR8>(l); l = xrow_sum(l);
  if ((lane >> 5) == ((lane >> 3) & 1)) {
    LAS float* dst = reinterpret_cast<LAS float*>(lds + L_PART) + ((seg * 4 + w) * 2 + (lane >> 5)) * DH + 8 * (lane & 7) + 4 * ((lane >> 4) & 1);
#pragma unroll
    for (int t = 0; t < 3; ++t) *reinterpret_cast<LAS f32x4*>(dst + 64 * t) = s.acc[t];
  }
  if (lane == 0) *reinterpret_cast<LAS f32x2*>(lds + L_ML + (seg * 4 + w) * 8) = f32x2{s.m, l};
  lds_arrive(reinterpret_cast<LAS unsigned*>(lds + L_CNT) + 2 + seg, lane);
}
__device__ __forceinline__ void oproj_col(const u32x4 (&W)[12], LAS unsigned char* lds, rsrc_t rws, unsigned yoff, u32x4 mask, int lane) {
  f32x4 acc[2] = {f32x4{0.f, 0.f, 0.f, 0.f}, f32x4{0.f, 0.f, 0.f, 0.f}};
  u32x4 o[2][3];
#pragma unroll
  for (int b = 0; b < 2; ++b)
#pragma unroll
    for (int i = 0; i < 3; ++i) o[b][i] = *reinterpret_cast<LAS u32x4*>(lds + L_O + ((b * 8 + (lane >> 3)) * 12 + 4 * i) * 4);
#pragma unroll
  for (int u = 0; u < 12; ++u)
#pragma unroll
    for (int b = 0; b < 2; ++b) acc[b] = mfma(afrag(o[b][u >> 2][u & 3], mask), W[u], acc[b]);
  f32x4 r;
#pragma unroll
  for (int e = 0; e < 4; ++e) {
    auto s = __builtin_amdgcn_permlane32_swap(__float_as_uint(acc[0][e]), __float_as_uint(acc[1][e]), false, false);
    const float lo = __uint_as_float(s[0]), hi = __uint_as_float(s[1]);
    r[e] = lo + dpp<ROR8>(hi);
  }
  stsc(canon4(r), rws, ((lane >> 3) & 1) == 0 ? (lane >> 5) * 1280 + (8 * (lane & 7) + 4 * ((lane >> 4) & 1)) * 4 : OOB, yoff);
}

#define STAMP(i) do { } while (0)

__global__ void __launch_bounds__(NTHR) fused_decode(Args a) {
  extern __shared__ __attribute__((aligned(16))) unsigned char smem[];
  LAS unsigned char* lds = (LAS unsigned char*)smem;
  const int tid = threadIdx.x, lane = tid & 63, w = __builtin_amdgcn_readfirstlane(tid >> 6);
  const int h = blockIdx.x & 31, j = blockIdx.x >> 5;
  const int pj = j >> 2;
  LAS unsigned* cnt = reinterpret_cast<LAS unsigned*>(lds + L_CNT);
  STAMP(0);
  const u32x4 mask = lane_mask(lane);
  const rsrc_t rws = mkrsrc(a.ws, WS_END);
  const int bx = BSWAP ? (h & 1) : 0;
  const int rh = (j >> 1) & 1, ch = j & 1;
  const rsrc_t rwo = mkrsrc(pj ? a.wo[1] : a.wo[0], WQB);
  const unsigned wo_voff = (lane >> 3) * (DM * 2) + (lane & 7) * 16;
  const unsigned wo_soff = (h * DH + rh * 96) * (DM * 2) + ch * 640 + w * 128;
  const unsigned yoff = WS_Y + (((pj * 2 + ch) * 64 + h * 2 + rh) * 640 + w * 64) * 4;

  if (w < 4) {
    const rsrc_t rwq = mkrsrc(pj ? a.wq[1] : a.wq[0], WQB);
    const rsrc_t rx = mkrsrc(pj ? a.x[1] : a.x[0], 2 * DM * 2);
    const rsrc_t rka = mkrsrc(a.k[0], KVB), rva = mkrsrc(a.v[0], KVB), rkb = mkrsrc(a.k[1], KVB), rvb = mkrsrc(a.v[1], KVB);
    const int row0 = (j & 3) * 160 + w * 40;
    const unsigned wq_voff = (lane >> 3) * (NH * ROWB) + (lane & 7) * 16;
    const unsigned wq_soff = row0 * (NH * ROWB) + h * ROWB;
    const unsigned kv_voff = (lane >> 3) * ROWB + (lane & 7) * 16;
    const unsigned tile_off = (j * 4 + w) * TILEB;
    u32x4 W[5][3], T[16][3], Kb[2][3], Vb[2][3];
#define LDW(i) _Pragma("unroll") for (int t = 0; t < 3; ++t) W[i][t] = ldnt(rwq, wq_voff + t * 128, wq_soff + (i) * 8 * (NH * ROWB));
#define BOFF(n) ((unsigned)((((n) >> 2) ^ bx) * NH + h) * SEGB + 3 * (SEGB / 4) + (((n) + h * STG) & 3) * RROW + tile_off)
#define AOFF(n) ((unsigned)((((n) >> 4) ^ bx) * NH + h) * SEGB + (((n) + h * STG) & 15) * RROW + tile_off)
#define LDT(i) _Pragma("unroll") for (int t = 0; t < 3; ++t) T[i][t] = ldnt(((i) & 1) ? rvb : rkb, kv_voff + t * 128, BOFF((i) >> 1));
    unsigned xv[2][5];
#pragma unroll
    for (int b = 0; b < 2; ++b)
#pragma unroll
      for (int u = 0; u < 5; ++u) xv[b][u] = __builtin_amdgcn_raw_buffer_load_b16(rx, (lane >> 3) * 2 + u * 16 + b * (DM * 2), row0 * 2, 0);
    SB();
#pragma unroll
    for (int u = 0; u < WQBURST; ++u) { LDW(u) SB(); }
    if (tid < 8) cnt[tid] = 0u;
    __syncthreads();
    {
      f32x4 qa[2][3];
#pragma unroll
      for (int b = 0; b < 2; ++b)
#pragma unroll
        for (int t = 0; t < 3; ++t) qa[b][t] = f32x4{0.f, 0.f, 0.f, 0.f};
#define QSTEP(u) { \
        WAITVM(3 * (((u) + 2 > WQBURST ? (u) + 2 : WQBURST) - (u) - 1)); SB(); \
        if ((u) + 2 >= WQBURST) { if ((u) + 2 < 5) { LDW((u) + 2 < 5 ? (u) + 2 : 0) } else { LDT((u) + 2 >= 5 ? (u) + 2 - 5 : 0) } } \
        SB(); \
        _Pragma("unroll") for (int b = 0; b < 2; ++b) { \
          const u32x4 A = afrag(xv[b][u] * 0x10001u, mask); \
          _Pragma("unroll") for (int t = 0; t < 3; ++t) qa[b][t] = mfma(A, W[u][t], qa[b][t]); } \
        SB(); }
      QSTEP(0) QSTEP(1) QSTEP(2) QSTEP(3) QSTEP(4)
      if ((lane >> 5) == ((lane >> 3) & 1)) {
#pragma unroll
        for (int b = 0; b < 2; ++b)
#pragma unroll
          for (int t = 0; t < 3; ++t)
            *reinterpret_cast<LAS f32x4*>(lds + L_QPART + (((w * 2 + b) * 2 + (lane >> 5)) * DH + 64 * t + 8 * (lane & 7) + 4 * ((lane >> 4) & 1)) * 4) = qa[b][t];
      }
      lds_arrive(cnt + 0, lane);
    }
    STAMP(1);
#pragma unroll
    for (int i = 2; i < 16; ++i) { SB(); WAITVM(3); SB(); LDT(i) }
    SB();
    STAMP(2);
    lds_wait<1>(cnt + 1, 1u);
    STAMP(3);
    Soft s; u32x4 q[3];
#define LDQ(seg) _Pragma("unroll") for (int t = 0; t < 3; ++t) q[t] = *reinterpret_cast<LAS u32x4*>(lds + L_Q + ((seg) * DH + 64 * t + 8 * (lane & 7)) * 2);
#define LDKA(sl, n) _Pragma("unroll") for (int t = 0; t < 3; ++t) Kb[sl][t] = ldnt(rka, kv_voff + t * 128, AOFF(n));
#define LDVA(sl, n) _Pragma("unroll") for (int t = 0; t < 3; ++t) Vb[sl][t] = ldnt(rva, kv_voff + t * 128, AOFF(n));
    LDQ(2 + bx)
    block4(s, T, q, mask);
    soft_flush(s, lds, 2 + bx, w, lane);
    LDQ(2 + (1 ^ bx))
    SB(); WAITVM(3); SB(); LDKA(0, 0) SB();
    WAITVM(3); SB(); LDVA(0, 0) SB();
    block4(s, T + 8, q, mask);
    soft_flush(s, lds, 2 + (1 ^ bx), w, lane);
    STAMP(4);
    for (int sg = 0; sg < 2; ++sg) {
      soft_reset(s); LDQ(sg ^ bx)
      const int last = sg == 1 ? 14 : 16;
      for (int r = 0; r < last; r += 2) {
        const int n = sg * 16 + r;
        SB(); WAITVM(3); SB(); LDKA(1, n + 1) SB();
        u32x4 A = step_qk(s, Kb[0], q, mask);
        SB(); WAITVM(3); SB(); LDVA(1, n + 1) SB();
        step_pv(s, A, Vb[0]);
        SB(); WAITVM(3); SB(); LDKA(0, n + 2) SB();
        A = step_qk(s, Kb[1], q, mask);
        SB(); WAITVM(3); SB(); LDVA(0, n + 2) SB();
        step_pv(s, A, Vb[1]);
      }
      if (sg == 0) soft_flush(s, lds, bx, w, lane);
    }
    u32x4 WO[12];
#define LDO(i) _Pragma("unroll") for (int t = 0; t < 3; ++t) WO[3 * (i) + t] = ldnt(rwo, wo_voff, wo_soff + (3 * (i) + t) * 8 * (DM * 2));
    {
      SB(); WAITVM(3); SB(); LDKA(1, 31) SB();
      u32x4 A = step_qk(s, Kb[0], q, mask);
      SB(); WAITVM(3); SB(); LDVA(1, 31) SB();
      step_pv(s, A, Vb[0]);
      SB(); WAITVM(3); SB(); LDO(0) SB();
      A = step_qk(s, Kb[1], q, mask);
      SB(); WAITVM(3); SB(); LDO(1) SB();
      step_pv(s, A, Vb[1]);
      soft_flush(s, lds, 1 ^ bx, w, lane);
      STAMP(5);
      SB(); WAITVM(3); SB(); LDO(2) SB();
      SB(); WAITVM(3); SB(); LDO(3) SB();
    }
    lds_wait<0>(cnt + 6, 1u);
    STAMP(6);
    oproj_col(WO, lds, rws, yoff, mask, lane);
    STAMP(7);
  } else {
    const unsigned long long ep = __hip_atomic_load(reinterpret_cast<const unsigned long long*>(a.ws + WS_EPOCH), __ATOMIC_RELAXED, __HIP_MEMORY_SCOPE_AGENT);
    const unsigned po_voff = lane < 48 ? lane * 16 : OOB;
    {
      const u32x4 e4 = {EMPTY, EMPTY, EMPTY, EMPTY};
#pragma unroll
      for (int sg = 0; sg < 4; ++sg) stsc(e4, rws, po_voff, WS_PO + ((h * 4 + sg) * 8 + j) * (DH * 4));
      __builtin_amdgcn_raw_buffer_store_b64(u32x2{EMPTY, EMPTY}, rws, lane < 4 ? lane * 64 : OOB, WS_ML + (h * 4 * 8 + j) * 8, AUX_SC1);
      const unsigned y0 = WS_Y + ((pj * 2 + ch) * 64 + h * 2 + rh) * 2560;
      stsc(e4, rws, lane * 16, y0); stsc(e4, rws, lane * 16 + 1024, y0);
      stsc(e4, rws, lane < 32 ? lane * 16 + 2048 : OOB, y0);
    }
    __syncthreads();
    const unsigned long long tag = ep + 0x9E3779B97F4A7C15ull;
    const unsigned tlo = (unsigned)tag, thi = (unsigned)(tag >> 32);
    lds_wait<1>(cnt + 0, 4u);
    STAMP(1);
    {
#pragma unroll
      for (int i = 0; i < 2; ++i) {
        const int qd = lane + 64 * i, qc = qd < 96 ? qd : 0;
        const int b = qc / 48, d = (qc - b * 48) * 4;
        f32x4 sum = {0.f, 0.f, 0.f, 0.f};
#pragma unroll
        for (int ww = 0; ww < 4; ++ww)
#pragma unroll
          for (int hf = 0; hf < 2; ++hf) sum += *reinterpret_cast<LAS f32x4*>(lds + L_QPART + (((ww * 2 + b) * 2 + hf) * DH + d) * 4);
        stsc(__builtin_bit_cast(u32x4, sum), rws, qd < 96 ? qd * 16 : OOB, WS_QP + (h * 8 + j) * (2 * DH * 4));
      }
      asm volatile("s_waitcnt vmcnt(0)" ::: "memory");
      if (lane == 0) __hip_atomic_store(reinterpret_cast<unsigned long long*>(a.ws + WS_FLAG) + h * 8 + j, tag, __ATOMIC_RELAXED, __HIP_MEMORY_SCOPE_AGENT);
    }
    STAMP(2);
    for (unsigned spin = 0; spin < SPIN_CAP; ++spin) {
      CLOBBER();
      const u32x2 f = __builtin_amdgcn_raw_buffer_load_b64(rws, (lane & 7) * 8, WS_FLAG + h * 64, AUX_SC1);
      if (__builtin_amdgcn_ballot_w64(((f[0] ^ tlo) | (f[1] ^ thi)) != 0u) == 0) break;
    }
    CLOBBER();
    STAMP(3);
    {
      u32x4 v[3][4];
#pragma unroll
      for (int i = 0; i < 3; ++i) {
        const int qd = lane + 64 * i, sg = qd / 48, d4 = qd - sg * 48;
#pragma unroll
        for (int qt = 0; qt < 4; ++qt) v[i][qt] = ldsc(rws, (sg >> 1) * (4 * 1536) + (sg & 1) * 768 + d4 * 16 + qt * 1536, WS_QP + h * 8 * 1536);
      }
#pragma unroll
      for (int i = 0; i < 3; ++i) {
        const f32x4 sum = (__builtin_bit_cast(f32x4, v[i][0]) + __builtin_bit_cast(f32x4, v[i][1])) + (__builtin_bit_cast(f32x4, v[i][2]) + __builtin_bit_cast(f32x4, v[i][3]));
        *reinterpret_cast<LAS u32x2*>(lds + L_Q + (lane + 64 * i) * 8) = u32x2{cvtpk(sum[0], sum[1]), cvtpk(sum[2], sum[3])};
      }
      lds_arrive(cnt + 1, lane);
    }
    STAMP(4);
    u32x4 WO[12];
#pragma unroll
    for (int u = 0; u < 12; ++u) WO[u] = ldnt(rwo, wo_voff, wo_soff + u * 8 * (DM * 2));
    for (unsigned spin = 0; spin < SPIN_CAP; ++spin) {
      CLOBBER();
      const u32x4 f0 = ldsc(rws, lane * 32, WS_FLAG), f1 = ldsc(rws, lane * 32 + 16, WS_FLAG);
      const unsigned bad = (f0[0] ^ tlo) | (f0[1] ^ thi) | (f0[2] ^ tlo) | (f0[3] ^ thi) | (f1[0] ^ tlo) | (f1[1] ^ thi) | (f1[2] ^ tlo) | (f1[3] ^ thi);
      if (__builtin_amdgcn_ballot_w64(bad != 0u) == 0) break;
      __builtin_amdgcn_s_sleep(8);
    }
    CLOBBER();
#pragma unroll
    for (int i = 0; i < 4; ++i) {
      const int sg = (i < 2 ? 2 : 0) + ((i & 1) ^ bx);
      if (i == 3) lds_wait<0>(cnt + 2 + sg, 4u); else lds_wait<4>(cnt + 2 + sg, 4u);
      const f32x4 ml0 = *reinterpret_cast<LAS f32x4*>(lds + L_ML + sg * 32), ml1 = *reinterpret_cast<LAS f32x4*>(lds + L_ML + sg * 32 + 16);
      const int d = (lane < 48 ? lane : 0) * 4;
      f32x4 pv[4];
#pragma unroll
      for (int ww = 0; ww < 4; ++ww)
        pv[ww] = *reinterpret_cast<LAS f32x4*>(lds + L_PART + (((sg * 4 + ww) * 2 + 0) * DH + d) * 4) + *reinterpret_cast<LAS f32x4*>(lds + L_PART + (((sg * 4 + ww) * 2 + 1) * DH + d) * 4);
      const float M = fmaxf(fmaxf(ml0[0], ml0[2]), fmaxf(ml1[0], ml1[2]));
      const float w0 = __builtin_amdgcn_exp2f(ml0[0] - M), w1 = __builtin_amdgcn_exp2f(ml0[2] - M), w2 = __builtin_amdgcn_exp2f(ml1[0] - M), w3 = __builtin_amdgcn_exp2f(ml1[2] - M);
      const float L = (w0 * ml0[1] + w1 * ml0[3]) + (w2 * ml1[1] + w3 * ml1[3]);
      const f32x4 o = (pv[0] * w0 + pv[1] * w1) + (pv[2] * w2 + pv[3] * w3);
      stsc(canon4(o), rws, po_voff, WS_PO + ((h * 4 + sg) * 8 + j) * (DH * 4));
      __builtin_amdgcn_raw_buffer_store_b64(u32x2{canon(M), canon(L)}, rws, lane == 0 ? 0u : OOB, WS_ML + ((h * 4 + sg) * 8 + j) * 8, AUX_SC1);
    }
    STAMP(5);
    {
      const int ln = lane < 48 ? lane : lane - 48, b = ln / 24, d4 = ln - b * 24;
      u32x4 v[8], ml[4];
      for (unsigned spin = 0; spin < SPIN_CAP; ++spin) {
        CLOBBER();
#pragma unroll
        for (int sp = 0; sp < 8; ++sp) v[sp] = ldsc(rws, b * (8 * DH * 4) + d4 * 16 + sp * (DH * 4), WS_PO + (h * 4 + pj * 2) * (8 * DH * 4) + rh * 384);
#pragma unroll
        for (int i = 0; i < 4; ++i) ml[i] = ldsc(rws, b * 64 + i * 16, WS_ML + (h * 4 + pj * 2) * 64);
        unsigned mx = 0u;
#pragma unroll
        for (int sp = 0; sp < 8; ++sp) mx = max4(v[sp], mx);
#pragma unroll
        for (int i = 0; i < 4; ++i) mx = max4(ml[i], mx);
        if (__builtin_amdgcn_ballot_w64(mx == EMPTY) == 0) break;
      }
      float M = -INFINITY;
#pragma unroll
      for (int i = 0; i < 4; ++i) M = fmaxf(M, fmaxf(__uint_as_float(ml[i][0]), __uint_as_float(ml[i][2])));
      float L = 0.f; f32x4 o = {0.f, 0.f, 0.f, 0.f};
#pragma unroll
      for (int sp = 0; sp < 8; ++sp) {
        const float wt = __builtin_amdgcn_exp2f(__uint_as_float(ml[sp >> 1][(sp & 1) * 2]) - M);
        L = fmaf(wt, __uint_as_float(ml[sp >> 1][(sp & 1) * 2 + 1]), L);
        o += __builtin_bit_cast(f32x4, v[sp]) * wt;
      }
      const float inv = 1.f / L;
      if (lane < 48) {
#pragma unroll
        for (int e = 0; e < 4; ++e) {
          const int r = d4 * 4 + e; const float ov = o[e] * inv;
          *reinterpret_cast<LAS unsigned*>(lds + L_O + ((b * 8 + (r & 7)) * 12 + (r >> 3)) * 4) = cvtpk(ov, ov);
        }
      }
      lds_arrive(cnt + 6, lane);
    }
    STAMP(6);
    oproj_col(WO, lds, rws, yoff, mask, lane);
    if (blockIdx.x < 160) {
      const int un = blockIdx.x, cg = un % 20, b = (un / 20) & 1, pc = un / 40;
      u32x4 v[4];
      for (unsigned spin = 0; spin < SPIN_CAP; ++spin) {
        CLOBBER();
#pragma unroll
        for (int i = 0; i < 4; ++i) v[i] = ldsc(rws, (lane >> 2) * 2560 + (lane & 3) * 16, WS_Y + (pc * 64 + 16 * i) * 2560 + b * 1280 + cg * 64);
        const unsigned mx = max4(v[0], max4(v[1], max4(v[2], max4(v[3], 0u))));
        if (__builtin_amdgcn_ballot_w64(mx == EMPTY) == 0) break;
      }
      f32x4 sum = (__builtin_bit_cast(f32x4, v[0]) + __builtin_bit_cast(f32x4, v[1])) + (__builtin_bit_cast(f32x4, v[2]) + __builtin_bit_cast(f32x4, v[3]));
#pragma unroll
      for (int e = 0; e < 4; ++e) { float x = sum[e]; x += dpp<ROR8>(x); x += dpp<ROR4>(x); sum[e] = xrow_sum(x); }
      if (lane < 4) *reinterpret_cast<u32x2*>(a.out + (b * 1280 + pc * 320 + cg * 16 + lane * 4) * 2) = u32x2{cvtpk(sum[0], sum[1]), cvtpk(sum[2], sum[3])};
      if (un == 0 && lane == 0) __hip_atomic_store(reinterpret_cast<unsigned long long*>(a.ws + WS_EPOCH), tag, __ATOMIC_RELAXED, __HIP_MEMORY_SCOPE_AGENT);
    }
    STAMP(7);
  }
}

extern "C" void kernel_launch(void* const* d_in, const int* in_sizes, int n_in,
                              void* d_out, int out_size, void* d_ws, size_t ws_size,
                              hipStream_t stream) {
  Args a;
  for (int p = 0; p < 2; ++p) {
    a.x[p] = (const unsigned char*)d_in[5 * p + 0];
    a.k[p] = (const unsigned char*)d_in[5 * p + 1];
    a.v[p] = (const unsigned char*)d_in[5 * p + 2];
    a.wq[p] = (const unsigned char*)d_in[5 * p + 3];
    a.wo[p] = (const unsigned char*)d_in[5 * p + 4];
  }
  a.out = (unsigned char*)d_out;
  a.ws = (unsigned char*)d_ws;
  (void)hipFuncSetAttribute((const void*)fused_decode, hipFuncAttributeMaxDynamicSharedMemorySize, LDS_BYTES);
  fused_decode<<<256, NTHR, LDS_BYTES, stream>>>(a);
}
```

```cpp
#include <hip/hip_runtime.h>
#include <hip/hip_bf16.h>
#include <stdint.h>

constexpr int STG = 32;
constexpr int ROT = 1;
constexpr int TAU = 446;
constexpr int TB0 = -900;
constexpr int ICH3 = 16;
constexpr int J0 = 68;
constexpr int QSLEEP = 16;

typedef unsigned u32x4 __attribute__((ext_vector_type(4)));
typedef unsigned u32x2 __attribute__((ext_vector_type(2)));
typedef float f32x4 __attribute__((ext_vector_type(4)));
typedef float f32x2 __attribute__((ext_vector_type(2)));
typedef short bf16x8 __attribute__((ext_vector_type(8)));
typedef __bf16 bf16x2 __attribute__((ext_vector_type(2)));
typedef __amdgpu_buffer_rsrc_t rsrc_t;
#define LAS __attribute__((address_space(3)))

constexpr int NH = 32, DH = 192, DM = 640, SK = 4096;
constexpr unsigned ROWB = DH * 2;
constexpr unsigned SEGB = SK * ROWB;
constexpr unsigned KVB = 2u * NH * SEGB;
constexpr unsigned WQB = DM * NH * ROWB;
constexpr unsigned TILEB = 8 * ROWB;
constexpr int NTHR = 320;
constexpr int AUX_NT = 2, AUX_SC1 = 16;
constexpr unsigned TMASK = 0x3FFu, QMASK = 0xFFu;
constexpr unsigned SPIN_CAP = 1u << 16;
constexpr float NEGBIG = -3.0e38f;

constexpr unsigned WS_EPOCH = 0;
constexpr unsigned WS_QP = 4096;
constexpr unsigned RECB = 896;
constexpr unsigned WS_PO = WS_QP + 256 * 2 * DH * 4;
constexpr unsigned WS_Y = WS_PO + NH * 4 * 8 * RECB;
constexpr unsigned WS_END = WS_Y + 4 * 64 * 640 * 4;
static_assert(WS_Y % 256 == 0 && WS_PO % 128 == 0, "");

constexpr int L_PART = 0;
constexpr int L_ML = L_PART + 4 * 4 * 2 * DH * 4;
constexpr int L_Q = L_ML + 4 * 4 * 8;
constexpr int L_O = L_Q + 4 * DH * 2;
constexpr int L_CNT = L_O + 2 * 8 * 12 * 4;
constexpr int L_QPART = L_CNT + 128;
constexpr int L_SC = L_QPART + 4 * 2 * 2 * DH * 4;
constexpr int L_DUMP = L_SC + 4 * 8 * 40 * 4;
constexpr int L_END = L_DUMP + 4096;
constexpr int LDS_BYTES = 96 * 1024;
static_assert(L_END <= LDS_BYTES, "");

struct Args {
  const unsigned char* wq[2];
  const unsigned char* x[2];
  const unsigned char* k[2];
  const unsigned char* v[2];
  const unsigned char* wo[2];
  unsigned char* out;
  unsigned char* ws;
};

__device__ __forceinline__ float dot2(unsigned a, unsigned b, float c) {
  return __builtin_amdgcn_fdot2_f32_bf16(__builtin_bit_cast(bf16x2, a), __builtin_bit_cast(bf16x2, b), c, false);
}
__device__ __forceinline__ unsigned cvtpk(float lo, float hi) {
  const f32x2 v = {lo, hi};
  return __builtin_bit_cast(unsigned, __builtin_convertvector(v, bf16x2));
}
template <int CTRL> __device__ __forceinline__ float dpp(float x) {
  return __builtin_bit_cast(float, __builtin_amdgcn_mov_dpp(__builtin_bit_cast(int, x), CTRL, 0xf, 0xf, true));
}
constexpr int XOR1 = 0xB1, XOR2 = 0x4E, HMIR = 0x141, ROR8 = 0x128, ROR4 = 0x124;
__device__ __forceinline__ float xrow_max(float x) {
  auto s = __builtin_amdgcn_permlane16_swap(__float_as_uint(x), __float_as_uint(x), false, false);
  x = fmaxf(__uint_as_float(s[0]), __uint_as_float(s[1]));
  auto t = __builtin_amdgcn_permlane32_swap(__float_as_uint(x), __float_as_uint(x), false, false);
  return fmaxf(__uint_as_float(t[0]), __uint_as_float(t[1]));
}
__device__ __forceinline__ float xrow_sum(float x) {
  auto s = __builtin_amdgcn_permlane16_swap(__float_as_uint(x), __float_as_uint(x), false, false);
  x = __uint_as_float(s[0]) + __uint_as_float(s[1]);
  auto t = __builtin_amdgcn_permlane32_swap(__float_as_uint(x), __float_as_uint(x), false, false);
  return __uint_as_float(t[0]) + __uint_as_float(t[1]);
}
__device__ __forceinline__ f32x4 mfma(u32x4 a, u32x4 b, f32x4 c) {
  return __builtin_amdgcn_mfma_f32_16x16x32_bf16(__builtin_bit_cast(bf16x8, a), __builtin_bit_cast(bf16x8, b), c, 0, 0, 0);
}
__device__ __forceinline__ u32x4 lane_mask(int lane) {
  const int j = lane & 7;
  const unsigned hm = (j & 1) ? 0xFFFF0000u : 0x0000FFFFu;
  u32x4 m;
  m[0] = (j >> 1) == 0 ? hm : 0u; m[1] = (j >> 1) == 1 ? hm : 0u; m[2] = (j >> 1) == 2 ? hm : 0u; m[3] = (j >> 1) == 3 ? hm : 0u;
  return m;
}
__device__ __forceinline__ u32x4 afrag(unsigned pp, u32x4 mask) {
  u32x4 a; a[0] = pp & mask[0]; a[1] = pp & mask[1]; a[2] = pp & mask[2]; a[3] = pp & mask[3]; return a;
}
__device__ __forceinline__ float bf16r(float x) { return __uint_as_float(cvtpk(x, x) << 16); }
__device__ __forceinline__ rsrc_t mkrsrc(const void* p, unsigned bytes) {
  return __builtin_amdgcn_make_buffer_rsrc(const_cast<void*>(p), 0, bytes, 0x00020000);
}
__device__ __forceinline__ u32x4 ldnt(rsrc_t r, unsigned voff, unsigned soff) { return __builtin_amdgcn_raw_buffer_load_b128(r, voff, soff, AUX_NT); }
__device__ __forceinline__ u32x4 ldsc(rsrc_t r, unsigned voff, unsigned soff) { return __builtin_amdgcn_raw_buffer_load_b128(r, voff, soff, AUX_SC1); }
__device__ __forceinline__ void stsc(u32x4 v, rsrc_t r, unsigned voff, unsigned soff) { __builtin_amdgcn_raw_buffer_store_b128(v, r, voff, soff, AUX_SC1); }
template <unsigned MASK> __device__ __forceinline__ u32x4 tag4(f32x4 v, u32x4 tg) {
  u32x4 r;
#pragma unroll
  for (int e = 0; e < 4; ++e) r[e] = (__float_as_uint(v[e]) & ~MASK) | (tg[e] & MASK);
  return r;
}
template <unsigned MASK> __device__ __forceinline__ float untag(unsigned u) { return __uint_as_float((u & ~MASK) | ((MASK + 1) >> 1)); }
template <unsigned MASK> __device__ __forceinline__ f32x4 untag4(u32x4 v) { return f32x4{untag<MASK>(v[0]), untag<MASK>(v[1]), untag<MASK>(v[2]), untag<MASK>(v[3])}; }
__device__ __forceinline__ unsigned bad4(u32x4 v, u32x4 tg, unsigned acc) {
#pragma unroll
  for (int e = 0; e < 4; ++e) acc = __builtin_amdgcn_bitop3_b32(v[e], tg[e], acc, 0xBE);
  return acc;
}
constexpr unsigned OOB = 0x80000000u;
#define SB() do { asm volatile("" ::: "memory"); __builtin_amdgcn_sched_barrier(0); } while (0)
#define WAITVM(n) __builtin_amdgcn_s_waitcnt(0x0F70 | ((n) & 15) | (((n) >> 4) << 14))
#define CLOBBER() asm volatile("" ::: "memory")

__device__ __forceinline__ void lds_arrive(LAS unsigned char* lds, int word, int lane, int z0) {
  CLOBBER();
  __hip_atomic_fetch_add(reinterpret_cast<LAS unsigned*>(lds + L_DUMP + lane * 4 + ((L_CNT - L_DUMP + word * 4) & z0)), 1u, __ATOMIC_RELAXED, __HIP_MEMORY_SCOPE_WORKGROUP);
  CLOBBER();
}
template <int SLEEP> __device__ __forceinline__ void lds_wait(LAS unsigned* p, unsigned want) {
  while ((unsigned)__builtin_amdgcn_readfirstlane(__hip_atomic_load(p, __ATOMIC_RELAXED, __HIP_MEMORY_SCOPE_WORKGROUP)) < want) __builtin_amdgcn_s_sleep(SLEEP);
  CLOBBER();
}

__device__ __forceinline__ float score(const u32x4 (&K)[3], const u32x4 (&q)[3]) {
  constexpr float SCALE = 0.07216878364870322f, LOG2E = 1.4426950408889634f;
  float d0 = 0.f, d1 = 0.f;
#pragma unroll
  for (int t = 0; t < 3; ++t) { d0 = dot2(K[t][0], q[t][0], d0); d1 = dot2(K[t][1], q[t][1], d1); d0 = dot2(K[t][2], q[t][2], d0); d1 = dot2(K[t][3], q[t][3], d1); }
  float d = d0 + d1;
  d += dpp<XOR1>(d); d += dpp<XOR2>(d); d += dpp<HMIR>(d);
  return bf16r(bf16r(d) * SCALE) * LOG2E;
}
__device__ __forceinline__ float pin(float x) { asm volatile("" : "+v"(x)); return x; }
__device__ __forceinline__ float wave_max(float x) { x = fmaxf(x, dpp<ROR8>(x)); return xrow_max(x); }
__device__ __forceinline__ unsigned now10() { return (unsigned)__builtin_amdgcn_s_memrealtime(); }
__device__ __forceinline__ void pace(unsigned tprev, unsigned tgt) {
  if ((int)(tprev - tgt) < 0) { while ((int)(now10() - tgt) < 0) __builtin_amdgcn_s_sleep(1); }
}
__device__ __forceinline__ void step_pv(f32x4 (&acc)[3], unsigned pp, const u32x4 (&V)[3], u32x4 mask) {
  const u32x4 A = afrag(pp, mask);
#pragma unroll
  for (int t = 0; t < 3; ++t) acc[t] = mfma(A, V[t], acc[t]);
  asm volatile("" : "+v"(acc[0]), "+v"(acc[1]), "+v"(acc[2]));
}
__device__ __forceinline__ u32x4 probs(f32x4 sc, float m, float& l) {
  u32x4 r;
#pragma unroll
  for (int e = 0; e < 4; ++e) { const float p = __builtin_amdgcn_exp2f(sc[e] - m); l += p; r[e] = cvtpk(p, p); }
  return r;
}
__device__ __forceinline__ void put_ml(LAS unsigned char* lds, int seg, int w, int lane, int z0, float m, float l) {
  l += dpp<ROR8>(l); l = xrow_sum(l);
  *reinterpret_cast<LAS f32x2*>(lds + L_DUMP + 2048 + lane * 8 + ((L_ML - L_DUMP - 2048 + (seg * 4 + w) * 8) & z0)) = f32x2{m, l};
  lds_arrive(lds, 8 + seg, lane, z0);
}
__device__ __forceinline__ u32x4 oproj_one(const u32x4 (&W)[12], LAS unsigned char* lds, u32x4 mask, u32x4 tg, int lane, int b) {
  u32x4 o[3];
#pragma unroll
  for (int k = 0; k < 3; ++k) o[k] = *reinterpret_cast<LAS u32x4*>(lds + L_O + ((b * 8 + (lane >> 3)) * 12 + 4 * k) * 4);
  SB();
  f32x4 acc0 = {0.f, 0.f, 0.f, 0.f}, acc1 = {0.f, 0.f, 0.f, 0.f};
#pragma unroll
  for (int u = 0; u < 12; u += 2) {
    acc0 = mfma(afrag(o[u >> 2][u & 3], mask), W[u], acc0);
    acc1 = mfma(afrag(o[u >> 2][(u & 3) + 1], mask), W[u + 1], acc1);
  }
  acc0 += acc1;
  f32x4 r;
#pragma unroll
  for (int e = 0; e < 4; ++e) {
    auto s = __builtin_amdgcn_permlane32_swap(__float_as_uint(acc0[e]), __float_as_uint(acc0[e]), false, false);
    r[e] = __uint_as_float(s[0]) + dpp<ROR8>(__uint_as_float(s[1]));
  }
  return tag4<TMASK>(r, tg);
}

__global__ void __launch_bounds__(NTHR, 2) fused_decode(Args a) {
  extern __shared__ __attribute__((aligned(16))) unsigned char smem[];
  LAS unsigned char* lds = (LAS unsigned char*)smem;
  const int tid = threadIdx.x, lane = tid & 63, w = __builtin_amdgcn_readfirstlane(tid >> 6);
  const unsigned char *pwq0 = a.wq[0], *pwq1 = a.wq[1], *px0 = a.x[0], *px1 = a.x[1], *pk0 = a.k[0], *pk1 = a.k[1], *pv0 = a.v[0], *pv1 = a.v[1], *pwo0 = a.wo[0], *pwo1 = a.wo[1];
  unsigned char *pout = a.out, *pws = a.ws;
  asm volatile("" : "+s"(pwq0), "+s"(pwq1), "+s"(px0), "+s"(px1), "+s"(pk0), "+s"(pk1), "+s"(pv0), "+s"(pv1), "+s"(pwo0), "+s"(pwo1), "+s"(pout), "+s"(pws));
  const int h = blockIdx.x & 31, j = blockIdx.x >> 5;
  const int pj = j >> 2;
  LAS unsigned* cnt = reinterpret_cast<LAS unsigned*>(lds + L_CNT);
  const int rh = (j >> 1) & 1, ch = j & 1;
  const int z0 = -(int)(lane == 0);
  const unsigned y_voff = (lane & 0x28) == 0 ? (8 * (lane & 7) + 4 * ((lane >> 4) & 1)) * 4 : OOB;

  if (__builtin_expect(w < 4, 1)) {
    const rsrc_t rwq = mkrsrc(pj ? pwq1 : pwq0, WQB);
    const int row0 = (j & 3) * 160 + w * 40;
    const unsigned wq_voff = (lane >> 3) * (NH * ROWB) + (lane & 7) * 16;
    const unsigned wq_soff = row0 * (NH * ROWB) + h * ROWB;
    u32x4 W[5][3], T[8][3], R[4][3];
#define LDW(i) _Pragma("unroll") for (int t = 0; t < 3; ++t) W[i][t] = ldnt(rwq, wq_voff + t * 128, wq_soff + (i) * 8 * (NH * ROWB));
    LDW(0) SB();
    const rsrc_t rx = mkrsrc(pj ? px1 : px0, 2 * DM * 2);
    unsigned xv[2][5];
#pragma unroll
    for (int b = 0; b < 2; ++b)
#pragma unroll
      for (int u = 0; u < 5; ++u) xv[b][u] = __builtin_amdgcn_raw_buffer_load_b16(rx, (lane >> 3) * 2 + u * 16 + b * (DM * 2), row0 * 2, 0);
    SB();
#pragma unroll
    for (int u = 1; u < 5; ++u) { LDW(u) SB(); }
    const u32x4 mask = lane_mask(lane);
    const int vm = -(int)((lane >> 5) == ((lane >> 3) & 1));
    const int pbase = ((((w * 2 + (lane >> 5)) * DH + 8 * (lane & 7) + 4 * ((lane >> 4) & 1)) * 4) & vm) | ((L_DUMP + lane * 16) & ~vm);
    const unsigned kv_voff = (lane >> 3) * ROWB + (lane & 7) * 16;
#define BOFF(n) ((unsigned)(((n) >> 2) * NH + h) * SEGB + 3 * (SEGB / 4) + (((n) * 32 + ((j + ROT * (n)) & 7) * 4 + w + h * STG) & 127) * TILEB)
#define LDT(i) _Pragma("unroll") for (int t = 0; t < 3; ++t) T[i][t] = ldnt(mkrsrc(pk1, KVB), kv_voff + t * 128, BOFF(i));
#define LDQ(seg) _Pragma("unroll") for (int t = 0; t < 3; ++t) q[t] = *reinterpret_cast<LAS u32x4*>(lds + L_Q + ((seg) * DH + 64 * t + 8 * (lane & 7)) * 2);
    const unsigned hoff = h * SEGB;
    const unsigned char* pwo = pj ? pwo1 : pwo0;
    const unsigned wo_soff = (h * DH + rh * 96) * (DM * 2) + ch * 640 + w * 128;
    const unsigned wo_voff = (lane >> 3) * (DM * 2) + (lane & 7) * 16;
    struct Blk { const unsigned char* p; unsigned n, base, c1, m1, s1, m2, d, v; };
    auto mkblk = [&](int i) {
      const bool b_ = i < 16, x_ = i >= 80;
      Blk r;
      r.p = b_ ? pv1 : (i < 48 ? pk0 : (x_ ? pwo : pv0));
      r.n = x_ ? WQB : KVB;
      const unsigned bat = b_ ? ((unsigned)i >> 2) & 1u : (((unsigned)i >> 4) + 1u) & 1u;
      r.base = x_ ? wo_soff : hoff + (b_ ? 3 * (SEGB / 4) : 0u) + bat * (NH * SEGB);
      r.c1 = x_ ? 0u : w + h * STG; r.m1 = (b_ || x_) ? 127u : 511u; r.s1 = x_ ? 24 * DM * 2 / 32 : TILEB; r.m2 = x_ ? 0u : 7u; r.d = x_ ? 8 * DM * 2 : 128u;
      r.v = x_ ? wo_voff : kv_voff;
      return r;
    };
#define LDR(sl, B, i) { const unsigned i_ = (i); \
      const unsigned o_ = B.base + ((i_ * 32 + ((j + ROT * i_) & B.m2) * 4 + B.c1) & B.m1) * B.s1; \
      const rsrc_t r_ = mkrsrc(B.p, B.n); \
      SB(); { const unsigned tg_ = B.m2 ? tbase + (i_ * TAU) / 10u : tclk; pace(tclk, tg_); if (i_ < ICH3 || (i_ >= J0 && i_ < 80)) WAITVM(6); else WAITVM(3); } SB(); \
      _Pragma("unroll") for (int t = 0; t < 3; ++t) R[sl][t] = ldnt(r_, B.v, o_ + t * B.d); \
      SB(); tclk = now10(); }
    unsigned tclk;
    {
      f32x4 qa[2][3];
#pragma unroll
      for (int b = 0; b < 2; ++b)
#pragma unroll
        for (int t = 0; t < 3; ++t) qa[b][t] = f32x4{0.f, 0.f, 0.f, 0.f};
#define QSTEP(u) { \
        WAITVM((u) < 3 ? 3 * (4 - (u)) : 3); SB(); \
        if ((u) >= 3) { LDT((u) >= 3 ? (u) - 3 : 0) } \
        SB(); \
        _Pragma("unroll") for (int b = 0; b < 2; ++b) { \
          const u32x4 A = afrag(xv[b][u] * 0x10001u, mask); \
          _Pragma("unroll") for (int t = 0; t < 3; ++t) qa[b][t] = mfma(A, W[u][t], qa[b][t]); } \
        SB(); }
      QSTEP(0) QSTEP(1) QSTEP(2) QSTEP(3)
      tclk = now10();
      QSTEP(4)
      {
        const int off = pbase + ((L_QPART + w * (2 * DH * 4)) & vm);
#pragma unroll
        for (int b = 0; b < 2; ++b)
#pragma unroll
          for (int t = 0; t < 3; ++t) *reinterpret_cast<LAS f32x4*>(lds + off + b * (2 * DH * 4) + 256 * t) = qa[b][t];
      }
      __syncthreads();
    }
    const unsigned tbase = tclk + (TB0) / 10;
#pragma unroll
    for (int i = 2; i < 8; ++i) { SB(); pace(tclk, tbase + (i * TAU) / 10u); WAITVM(3); SB(); LDT(i) SB(); tclk = now10(); }
    Blk nb = mkblk(8);
    LDR(0, nb, 8) LDR(1, nb, 9)
    lds_wait<1>(cnt + 1, 1u);
    LAS unsigned char* scp = lds + L_SC + (w * 8 + (lane >> 3)) * 160;
#define BSC(sg) { u32x4 q[3]; f32x4 sc; LDQ(2 + sg) \
      _Pragma("unroll") for (int u = 0; u < 4; ++u) sc[u] = score(T[4 * sg + u], q); \
      const float m_ = wave_max(fmaxf(fmaxf(fmaxf(sc[0], sc[1]), NEGBIG), fmaxf(sc[2], sc[3]))); float l_ = 0.f; \
      *reinterpret_cast<LAS u32x4*>(scp + 16 * sg) = probs(sc, m_, l_); \
      put_ml(lds, 2 + sg, w, lane, z0, m_, l_); }
    BSC(0)
    LDR(2, nb, 10)
    BSC(1)
    f32x4 acc[3];
#pragma unroll
    for (int t = 0; t < 3; ++t) acc[t] = f32x4{0.f, 0.f, 0.f, 0.f};
    u32x4 q[3] = {mask, mask, mask};
    float mx = NEGBIG;
#pragma clang loop unroll(disable)
    for (int i = 8; i < 80; i += 4) {
      const Blk cb = nb; nb = mkblk(i + 4);
      const bool isk = i >= 16 && i < 48;
      LAS unsigned char* sp = scp + (i < 48 ? i - 8 : i - 40) * 4;
      u32x4 pp = *reinterpret_cast<LAS u32x4*>(sp);
#define STEP(k) if (isk) pp[k] = __float_as_uint(pin(score(R[k], q))); else step_pv(acc, pp[k], R[k], mask);
      STEP(0) LDR(3, cb, i + 3)
      STEP(1) LDR(0, nb, i + 4)
      STEP(2) LDR(1, nb, i + 5)
      STEP(3)
      if (isk) {
        *reinterpret_cast<LAS u32x4*>(sp) = pp;
        mx = fmaxf(fmaxf(mx, fmaxf(__uint_as_float(pp[0]), __uint_as_float(pp[1]))), fmaxf(__uint_as_float(pp[2]), __uint_as_float(pp[3])));
        if ((i & 15) == 12) {
          f32x4 sv[4];
#pragma unroll
          for (int k = 0; k < 4; ++k) sv[k] = *reinterpret_cast<LAS f32x4*>(sp - 48 + 16 * k);
          mx = wave_max(mx);
          float l = 0.f;
#pragma unroll
          for (int k = 0; k < 4; ++k) *reinterpret_cast<LAS u32x4*>(sp - 48 + 16 * k) = probs(sv[k], mx, l);
          put_ml(lds, (i >> 4) - 1, w, lane, z0, mx, l);
          mx = NEGBIG;
          LDQ(1)
        }
      } else if (i < 16 || (i & 15) == 12) {
        const int seg = i < 16 ? i >> 2 : (i >> 4) - 3, off = pbase + ((L_PART + seg * (4 * 2 * DH * 4)) & vm);
#pragma unroll
        for (int t = 0; t < 3; ++t) *reinterpret_cast<LAS f32x4*>(lds + off + 256 * t) = acc[t];
        lds_arrive(lds, 2 + seg, lane, z0);
#pragma unroll
        for (int t = 0; t < 3; ++t) acc[t] = f32x4{0.f, 0.f, 0.f, 0.f};
        if (i == 12) { lds_wait<1>(cnt + 1, 2u); LDQ(0) }
      }
      LDR(2, nb, i + 6)
    }
    {
      const rsrc_t rwo = mkrsrc(pwo, WQB);
#pragma unroll
      for (int u = 9; u < 12; ++u) {
        if (u == 9) { SB(); WAITVM(3); SB(); }
        R[u / 3][u % 3] = ldnt(rwo, wo_voff, wo_soff + u * (8 * DM * 2));
      }
      SB();
      const u32x4 tg = *reinterpret_cast<LAS u32x4*>(lds + L_CNT + 64);
      const rsrc_t rws = mkrsrc(pws, WS_END);
      u32x4 WO[12];
#pragma unroll
      for (int u = 0; u < 12; ++u) WO[u] = R[u / 3][u % 3];
#pragma clang loop unroll(disable)
      for (int b = 0; b < 2; ++b) {
        lds_wait<0>(cnt + 6, b + 1);
        stsc(oproj_one(WO, lds, mask, tg, lane, b), rws, y_voff, WS_Y + (((pj * 2 + ch) * 64 + h * 2 + rh) * 640 + w * 64) * 4 + b * 1280);
      }
    }
  } else {
    const rsrc_t rws = mkrsrc(pws, WS_END);
    const u32x2 ep = __builtin_amdgcn_raw_buffer_load_b64(rws, 0, WS_EPOCH, AUX_SC1);
    cnt[lane & 15] = 0u;
    const unsigned y0 = WS_Y + ((pj * 2 + ch) * 64 + h * 2 + rh) * 2560;
    const unsigned long long tag = (((unsigned long long)ep[1] << 32) | ep[0]) + 0x9E3779B97F4A7C15ull;
    const unsigned tlo = (unsigned)tag, thi = (unsigned)(tag >> 32);
    const u32x4 tg = {tlo & TMASK, (tlo >> 10) & TMASK, (tlo >> 20) & TMASK, thi & TMASK};
    *reinterpret_cast<LAS u32x4*>(lds + L_CNT + 64) = tg;
    __syncthreads();
    {
      f32x4 pr[2][8];
#pragma unroll
      for (int i = 0; i < 2; ++i) {
        const int qd = lane + 64 * i, qc = qd < 96 ? qd : 0;
        const int base = L_QPART + (qc + (qc >= 48 ? 48 : 0)) * 16;
#pragma unroll
        for (int k = 0; k < 8; ++k) pr[i][k] = *reinterpret_cast<LAS f32x4*>(lds + base + ((k >> 1) * 4 + (k & 1)) * (DH * 4));
      }
      SB();
#pragma unroll
      for (int i = 0; i < 2; ++i) {
        const int qd = lane + 64 * i;
        const f32x4 sum = ((pr[i][0] + pr[i][1]) + (pr[i][2] + pr[i][3])) + ((pr[i][4] + pr[i][5]) + (pr[i][6] + pr[i][7]));
        stsc(tag4<QMASK>(sum, tg), rws, qd < 96 ? qd * 16 : OOB, WS_QP + (h * 8 + j) * (2 * DH * 4));
      }
    }
#pragma clang loop unroll(disable)
    for (int p = 1; p >= 0; --p) {
      u32x4 v[2][4];
#pragma clang loop unroll(disable)
      for (unsigned spin = 0; spin < SPIN_CAP; ++spin) {
        __builtin_amdgcn_s_sleep(QSLEEP);
        CLOBBER();
        unsigned bad = 0u;
#pragma unroll
        for (int i = 0; i < 2; ++i)
#pragma unroll
          for (int qt = 0; qt < 4; ++qt) v[i][qt] = ldsc(rws, (i ? (lane & 31) + 64 : lane) * 16 + qt * 1536, WS_QP + (h * 8 + p * 4) * 1536);
#pragma unroll
        for (int i = 0; i < 2; ++i)
#pragma unroll
          for (int qt = 0; qt < 4; ++qt) bad = bad4(v[i][qt], tg, bad);
        if (__builtin_amdgcn_ballot_w64((bad & QMASK) != 0u) == 0) break;
      }
#pragma unroll
      for (int i = 0; i < 2; ++i) {
        const f32x4 sum = (untag4<QMASK>(v[i][0]) + untag4<QMASK>(v[i][1])) + (untag4<QMASK>(v[i][2]) + untag4<QMASK>(v[i][3]));
        *reinterpret_cast<LAS u32x2*>(lds + L_Q + p * 768 + (i ? (lane & 31) + 64 : lane) * 8) = u32x2{cvtpk(sum[0], sum[1]), cvtpk(sum[2], sum[3])};
      }
      lds_arrive(lds, 1, lane, z0);
      __builtin_amdgcn_s_sleep(32);
    }
    const rsrc_t rwo = mkrsrc(pj ? pwo1 : pwo0, WQB);
    u32x4 WO[12];
#pragma unroll
    for (int u = 0; u < 12; ++u) {
      WO[u] = ldnt(rwo, (lane >> 3) * (DM * 2) + (lane & 7) * 16, (h * DH + rh * 96 + u * 8) * (DM * 2) + ch * 640 + 4 * 128);
    }
    const u32x4 mask = lane_mask(lane);
    const int om = -(int)(lane < 24);
    const int obase = ((L_O + ((lane & 1) * 4 * 12 + (lane >> 1)) * 4) & om) | ((L_DUMP + lane * 4) & ~om);
    const int mbase = L_PART + (lane < 48 ? lane : 0) * 16;
    const unsigned rec0 = WS_PO + (h * 4 * 8 + j) * RECB;
    const unsigned yd = ((lane & 31) < 24 ? (lane & 31) : 0) * 16;
    unsigned yh_voff[4], ml_voff[8];
#pragma unroll
    for (int sp = 0; sp < 4; ++sp) yh_voff[sp] = ((j + (lane >> 5) * 4 + sp) & 7) * RECB + yd;
#pragma unroll
    for (int sp = 0; sp < 8; ++sp) ml_voff[sp] = (lane >> 5) * (8 * RECB) + ((j + sp) & 7) * RECB;
    u32x4 own = tg;
    const unsigned po_voff = lane < 56 ? lane * 16 : OOB, pd_voff = lane < 48 ? lane * 16 : OOB, pm_voff = (lane >= 48 && lane < 56) ? lane * 16 : OOB;
    u32x4 ra = tg, rb = tg, ya = tg, yb = tg;
    f32x4 wc = {0.f, 0.f, 0.f, 0.f}, wn = wc, sa = wc, sb = wc;
    float Mc = 0.f, Lc = 0.f, Mn = 0.f, Ln = 0.f;
#pragma clang loop unroll(disable)
    for (int s = 0; s < 7; ++s) {
      if (s <= 3) {
        const int sg = (s + 2) & 3;
        lds_wait<2>(cnt + 8 + sg, 4u);
        const f32x4 ml0 = *reinterpret_cast<LAS f32x4*>(lds + L_ML + sg * 32), ml1 = *reinterpret_cast<LAS f32x4*>(lds + L_ML + sg * 32 + 16);
        Mn = fmaxf(fmaxf(ml0[0], ml0[2]), fmaxf(ml1[0], ml1[2]));
        wn = f32x4{__builtin_amdgcn_exp2f(ml0[0] - Mn), __builtin_amdgcn_exp2f(ml0[2] - Mn), __builtin_amdgcn_exp2f(ml1[0] - Mn), __builtin_amdgcn_exp2f(ml1[2] - Mn)};
        Ln = (wn[0] * ml0[1] + wn[1] * ml0[3]) + (wn[2] * ml1[1] + wn[3] * ml1[3]);
      }
      if ((s >= 1 && s <= 3) || s == 6) {
        const int sg = s == 6 ? 1 : (s + 1) & 3;
        lds_wait<0>(cnt + 2 + sg, 4u);
        f32x4 pv[8];
#pragma unroll
        for (int k = 0; k < 8; ++k) pv[k] = *reinterpret_cast<LAS f32x4*>(lds + mbase + sg * (8 * DH * 4) + k * (DH * 4));
        SB();
        f32x4 o = ((pv[0] + pv[1]) * wc[0] + (pv[2] + pv[3]) * wc[1]) + ((pv[4] + pv[5]) * wc[2] + (pv[6] + pv[7]) * wc[3]);
        asm volatile("" : "+v"(o));
        o[0] = lane < 48 ? o[0] : Mc; o[1] = lane < 48 ? o[1] : Lc; o[2] = lane < 48 ? o[2] : __uint_as_float(__float_as_uint(Mc) << 10);
        const u32x4 rc = tag4<TMASK>(o, tg);
        stsc(rc, rws, s == 6 ? pd_voff : (s == 3 ? po_voff : OOB), rec0 + sg * (8 * RECB));
        *reinterpret_cast<LAS u32x4*>(lds + L_QPART + lane * 16) = rc; own = *reinterpret_cast<LAS u32x4*>(lds + L_QPART + rh * 384 + yd);
        if (s == 3) {
          stsc(ra, rws, po_voff, rec0 + 2 * (8 * RECB)); stsc(rb, rws, po_voff, rec0 + 3 * (8 * RECB));
          stsc(tag4<TMASK>(f32x4{Mn, Ln, __uint_as_float(__float_as_uint(Mn) << 10), Ln}, tg), rws, pm_voff, rec0 + 1 * (8 * RECB));
          __builtin_amdgcn_s_sleep(64);
        }
        ra = rb; rb = rc;
      }
      if (s <= 3) { wc = wn; Mc = Mn; Lc = Ln; }
      const int c = s >= 5;
      const unsigned recs = WS_PO + (h * 4 + pj * 2 + c) * (8 * RECB);
      if (s == 4) {
        u32x4 ml[8];
#pragma clang loop unroll(disable)
        for (unsigned spin = 0; spin < SPIN_CAP; ++spin) {
          CLOBBER();
#pragma unroll
          for (int sp = 0; sp < 8; ++sp) ml[sp] = ldsc(rws, ml_voff[sp], recs + 768);
          unsigned bad = 0u;
#pragma unroll
          for (int sp = 0; sp < 8; ++sp) bad = bad4(ml[sp], tg, bad);
          if (__builtin_amdgcn_ballot_w64((bad & TMASK) != 0u) == 0) break;
          __builtin_amdgcn_s_sleep(16);
        }
        float M = NEGBIG, ms[8];
#pragma unroll
        for (int sp = 0; sp < 8; ++sp) { ms[sp] = __uint_as_float((ml[sp][0] & ~TMASK) | ((ml[sp][2] >> 10) & TMASK)); M = fmaxf(M, ms[sp]); }
        float L = 0.f, wt[8];
#pragma unroll
        for (int sp = 0; sp < 8; ++sp) { wt[sp] = __builtin_amdgcn_exp2f(ms[sp] - M); L = fmaf(wt[sp], __uint_as_float(ml[sp][1] & ~TMASK), L); }
        const float inv = 1.f / L;
        sa = f32x4{wt[0], wt[1], wt[2], wt[3]} * inv; sb = f32x4{wt[4], wt[5], wt[6], wt[7]} * inv;
#pragma unroll
        for (int e = 0; e < 4; ++e) {
          auto r = __builtin_amdgcn_permlane32_swap(__float_as_uint(sa[e]), __float_as_uint(sb[e]), false, false);
          sa[e] = __uint_as_float(r[0]); sb[e] = __uint_as_float(r[1]);
        }
      }
      if (s == 4 || s == 6 - pj) {
        u32x4 v[4];
#pragma clang loop unroll(disable)
        for (unsigned spin = 0; spin < SPIN_CAP; ++spin) {
          CLOBBER();
#pragma unroll
          for (int sp = 0; sp < 4; ++sp) v[sp] = ldsc(rws, yh_voff[sp], recs + rh * 384);
          { const bool uo = s == 6 && lane < 32;
#pragma unroll
            for (int e = 0; e < 4; ++e) v[0][e] = uo ? own[e] : v[0][e]; }
          if (__builtin_amdgcn_ballot_w64((bad4(v[0], tg, bad4(v[1], tg, bad4(v[2], tg, bad4(v[3], tg, 0u)))) & TMASK) != 0u) == 0) break;
          if (s != 6) __builtin_amdgcn_s_sleep(16);
        }
        f32x4 o = (untag4<TMASK>(v[0]) * sa[0] + untag4<TMASK>(v[1]) * sa[1]) + (untag4<TMASK>(v[2]) * sa[2] + untag4<TMASK>(v[3]) * sa[3]);
#pragma unroll
        for (int e = 0; e < 4; ++e) {
          auto r = __builtin_amdgcn_permlane32_swap(__float_as_uint(o[e]), __float_as_uint(o[e]), false, false);
          o[e] = __uint_as_float(r[0]) + __uint_as_float(r[1]);
        }
        sa = sb;
        const int ob = obase + ((c * (8 * 12 * 4)) & om);
#pragma unroll
        for (int e = 0; e < 4; ++e) *reinterpret_cast<LAS unsigned*>(lds + ob + e * 48) = cvtpk(o[e], o[e]);
        lds_arrive(lds, 6, lane, z0);
        ya = yb; yb = oproj_one(WO, lds, mask, tg, lane, c);
      }
    }
    stsc(yb, rws, y_voff, y0 + 4 * 256 + 1280); stsc(ya, rws, y_voff, y0 + 4 * 256);
    if (blockIdx.x < 160) {
      const int un = blockIdx.x, cg = un % 20, b = (un / 20) & 1, pc = un / 40;
      u32x2 res = {0u, 0u};
      const unsigned yv = (lane >> 2) * 2560 + (lane & 3) * 16, ys = WS_Y + pc * 64 * 2560 + b * 1280 + cg * 64;
#pragma clang loop unroll(disable)
      for (unsigned spin = 0; spin < SPIN_CAP; ++spin) {
        CLOBBER();
        u32x4 v[4];
#pragma unroll
        for (int i = 0; i < 4; ++i) v[i] = ldsc(rws, yv, ys + i * 16 * 2560);
        const bool bad = __builtin_amdgcn_ballot_w64((bad4(v[0], tg, bad4(v[1], tg, bad4(v[2], tg, bad4(v[3], tg, 0u)))) & TMASK) != 0u) != 0;
        if (bad && spin) continue;
        f32x4 sum = (untag4<TMASK>(v[0]) + untag4<TMASK>(v[1])) + (untag4<TMASK>(v[2]) + untag4<TMASK>(v[3]));
#pragma unroll
        for (int e = 0; e < 4; ++e) { float x = sum[e]; x += dpp<ROR8>(x); x += dpp<ROR4>(x); sum[e] = xrow_sum(x); }
        res = u32x2{cvtpk(sum[0], sum[1]), cvtpk(sum[2], sum[3])};
        asm volatile("" : "+v"(res));
        if (!bad) break;
      }
      __builtin_amdgcn_raw_buffer_store_b64(res, mkrsrc(pout, 5120), lane < 4 ? lane * 8 : OOB, (b * 1280 + pc * 320 + cg * 16) * 2, AUX_SC1);
      if (un == 0) stsc(u32x4{tlo, thi, tlo, thi}, rws, lane < 8 ? lane * 16 : OOB, WS_EPOCH);
    }
  }
}

extern "C" void kernel_launch(void* const* d_in, const int* in_sizes, int n_in,
                              void* d_out, int out_size, void* d_ws, size_t ws_size,
                              hipStream_t stream) {
  Args a;
  for (int p = 0; p < 2; ++p) {
    a.x[p] = (const unsigned char*)d_in[5 * p + 0];
    a.k[p] = (const unsigned char*)d_in[5 * p + 1];
    a.v[p] = (const unsigned char*)d_in[5 * p + 2];
    a.wq[p] = (const unsigned char*)d_in[5 * p + 3];
    a.wo[p] = (const unsigned char*)d_in[5 * p + 4];
  }
  a.out = (unsigned char*)d_out;
  a.ws = (unsigned char*)d_ws;
  (void)hipFuncSetAttribute((const void*)fused_decode, hipFuncAttributeMaxDynamicSharedMemorySize, LDS_BYTES);
  fused_decode<<<256, NTHR, LDS_BYTES, stream>>>(a);
}
```
